```python
import torch

def _softmax(x, axis=-1):
    return torch.softmax(x, dim=axis)
def _gelu(x):
    return torch.nn.functional.gelu(x, approximate="tanh")
def _silu(x):
    return torch.nn.functional.silu(x)
def _sigmoid(x):
    return torch.sigmoid(x)
def _layernorm(x, axis=-1, eps=1e-5):
    m = x.mean(dim=axis, keepdim=True)
    v = x.var(dim=axis, keepdim=True, unbiased=False)
    return (x - m) / torch.sqrt(v + eps)
def _scatter_add(tgt, idx, src, axis=0):
    return tgt.clone().index_add_(axis, idx, src)
def _segment_sum(src, seg, n):
    out = torch.zeros((n,) + src.shape[1:], dtype=src.dtype)
    return out.index_add_(0, seg, src)
def _pad(x, pw):
    flat = tuple(v for pair in reversed(pw) for v in pair)
    return torch.nn.functional.pad(x, flat)

kernel_name = 'sequential_preLN__gqa_block_cached__swiglu_ffn__decode'

def setup_inputs():

    _w = (lambda a: (lambda t: t.to(torch.bfloat16) if t.is_floating_point() else t)(torch.as_tensor(a)))
    def _A_setup():
        B, Sq, D = 4, 1, 640
        Hq, Hkv, Sk, Dh = 32, 32, 4096, 192
        assert Hq % Hkv == 0, (Hq, Hkv)
        return {
            "x": _w((lambda *s: torch.randn(*s, dtype=torch.bfloat16) * 0.05)(B, Sq, D)),
            "k_cache": _w((lambda *s: torch.randn(*s, dtype=torch.bfloat16) * 3.2)(B, Hkv, Sk, Dh)),
            "v_cache": _w((lambda *s: torch.randn(*s, dtype=torch.bfloat16) * 0.4)(B, Hkv, Sk, Dh)),
            "wq": _w((lambda *s: torch.randn(*s, dtype=torch.bfloat16) * 3.2)(D, Hq * Dh) * (D ** -0.5)),
            "wo": _w((lambda *s: torch.randn(*s, dtype=torch.bfloat16) * 0.4)(Hq * Dh, D) * ((Hq * Dh) ** -0.5)),
        }
    _A_in = _A_setup()

    def _B_setup():
        B, S, D, F = 4, 1, 640, 1536
        return {
            "x": _w((lambda *s: torch.randn(*s, dtype=torch.bfloat16) * 0.05)(B, S, D)),
            "w_gate": _w((lambda *s: torch.randn(*s, dtype=torch.bfloat16) * 0.05)(D, F)),
            "w_up": _w((lambda *s: torch.randn(*s, dtype=torch.bfloat16) * 0.05)(D, F)),
            "w_down": _w((lambda *s: torch.randn(*s, dtype=torch.bfloat16) * 0.05)(F, D)),
        }
    _B_in = _B_setup()

    return {
        "A_x": _A_in["x"],
        "A_k_cache": _A_in["k_cache"],
        "A_v_cache": _A_in["v_cache"],
        "A_wq": _A_in["wq"],
        "A_wo": _A_in["wo"],
        "B_x": _B_in["x"],
        "B_w_gate": _B_in["w_gate"],
        "B_w_up": _B_in["w_up"],
        "B_w_down": _B_in["w_down"],
    }

def reference(A_x, A_k_cache, A_v_cache, A_wq, A_wo, B_x, B_w_gate, B_w_up, B_w_down):
    def _A_ref(x, k_cache, v_cache, wq, wo):
        B, Sq, D = x.shape
        Hq, Dh = 32, 192
        Hkv = k_cache.shape[1]
        rep = Hq // Hkv
        q = (lambda x, *d: x.permute(*d))((x @ wq).reshape(B, Sq, Hq, Dh), 0, 2, 1, 3)
        k = (lambda x, n, axis: torch.repeat_interleave(x, n, dim=axis))(k_cache, rep, axis=1)
        v = (lambda x, n, axis: torch.repeat_interleave(x, n, dim=axis))(v_cache, rep, axis=1)
        scores = torch.einsum("bhqd,bhkd->bhqk", q, k) * (Dh ** -0.5)
        p = _softmax(scores, axis=-1)
        o = torch.einsum("bhqk,bhkd->bhqd", p, v)
        return (lambda x, *d: x.permute(*d))(o, 0, 2, 1, 3).reshape(B, Sq, Hq * Dh) @ wo

    def _B_ref(x, w_gate, w_up, w_down):
        g = _silu(x @ w_gate)
        u = x @ w_up
        return (g * u) @ w_down

    x = A_x
    h = x + _A_ref(_layernorm(x, axis=-1), A_k_cache, A_v_cache, A_wq, A_wo)
    h2 = h + _B_ref(_layernorm(h, axis=-1), B_w_gate, B_w_up, B_w_down)
    return h2
```

```cpp
#include <hip/hip_runtime.h>
#include <hip/hip_bf16.h>
#include <stdint.h>

#define MIMIC 0

typedef unsigned u32x4 __attribute__((ext_vector_type(4)));
typedef __bf16 bf16x2 __attribute__((ext_vector_type(2)));
typedef float f32x4 __attribute__((ext_vector_type(4)));

constexpr int B = 4, D = 640, H = 32, SK = 4096, DH = 192, F = 1536, HD = H * DH;
constexpr int NSPLIT = 2, KPS = SK / NSPLIT;

__device__ __forceinline__ float bf2f(unsigned short b) { return __builtin_bit_cast(float, (unsigned)b << 16); }
__device__ __forceinline__ float bflo(unsigned u) { return __builtin_bit_cast(float, u << 16); }
__device__ __forceinline__ float bfhi(unsigned u) { return __builtin_bit_cast(float, u & 0xffff0000u); }
__device__ __forceinline__ unsigned short f2bf(float f) { __bf16 b = (__bf16)f; return __builtin_bit_cast(unsigned short, b); }
__device__ __forceinline__ float rbf(float f) { return bf2f(f2bf(f)); }
__device__ __forceinline__ float dot2(unsigned k, unsigned q, float acc) {
  return __builtin_amdgcn_fdot2_f32_bf16(__builtin_bit_cast(bf16x2, k), __builtin_bit_cast(bf16x2, q), acc, false);
}
template <int CTRL> __device__ __forceinline__ float dpp(float x) {
  return __builtin_bit_cast(float, __builtin_amdgcn_mov_dpp(__builtin_bit_cast(int, x), CTRL, 0xf, 0xf, true));
}
constexpr int XOR1 = 0xB1, XOR2 = 0x4E, XOR7 = 0x141, XOR8 = 0x128;

__device__ __forceinline__ float wave_sum(float v) {
  for (int o = 32; o > 0; o >>= 1) v += __shfl_xor(v, o);
  return v;
}

__device__ __forceinline__ void ln_row(const float* in, float* out, int lane) {
  float v[10]; float s = 0.f;
#pragma unroll
  for (int i = 0; i < 10; ++i) { v[i] = in[lane + 64 * i]; s += v[i]; }
  s = wave_sum(s);
  const float mean = s * (1.f / D);
  float q = 0.f;
#pragma unroll
  for (int i = 0; i < 10; ++i) { const float d = v[i] - mean; q += d * d; }
  q = wave_sum(q);
  const float var = q * (1.f / D);
  const float mb = rbf(mean), vb = rbf(var);
  const float den = rbf(sqrtf(rbf(vb + 1e-5f)));
#pragma unroll
  for (int i = 0; i < 10; ++i) out[lane + 64 * i] = rbf(rbf(v[i] - mb) / den);
}

template <int KR>
__device__ __forceinline__ void gemv64(const float* xs  , const unsigned short* W, int ldw, float* red  , float* res  ) {
  const int t = threadIdx.x, c8 = t & 7, kg = t >> 3;
  float acc[4][8];
#pragma unroll
  for (int b = 0; b < 4; ++b)
#pragma unroll
    for (int j = 0; j < 8; ++j) acc[b][j] = 0.f;
#pragma unroll 4
  for (int k = kg; k < KR; k += 32) {
    const u32x4 w = *reinterpret_cast<const u32x4*>(W + (size_t)k * ldw + c8 * 8);
#pragma unroll
    for (int b = 0; b < 4; ++b) {
      const float x = xs[b * KR + k];
#pragma unroll
      for (int j = 0; j < 4; ++j) { acc[b][2 * j] = fmaf(x, bflo(w[j]), acc[b][2 * j]); acc[b][2 * j + 1] = fmaf(x, bfhi(w[j]), acc[b][2 * j + 1]); }
    }
  }
#pragma unroll
  for (int b = 0; b < 4; ++b)
#pragma unroll
    for (int j = 0; j < 8; ++j) red[(kg * 4 + b) * 64 + c8 * 8 + j] = acc[b][j];
  __syncthreads();
  { float s = 0.f;
    for (int g = 0; g < 32; ++g) s += red[g * 256 + t];
    res[t] = s; }
  __syncthreads();
}

__global__ void __launch_bounds__(256) k_qproj(const unsigned short* x, const unsigned short* wq, unsigned short* q) {
  __shared__ float xs[4 * D], xn[4 * D], red[32 * 256], res[256];
  const int t = threadIdx.x;
  for (int i = t; i < 4 * D; i += 256) xs[i] = bf2f(x[i]);
  __syncthreads();
  ln_row(xs + (t >> 6) * D, xn + (t >> 6) * D, t & 63);
  __syncthreads();
  gemv64<D>(xn, wq + blockIdx.x * 64, HD, red, res);
  q[(t >> 6) * HD + blockIdx.x * 64 + (t & 63)] = f2bf(res[t]);
}

constexpr int AW = 8;
constexpr int TT = 2;
__global__ void __launch_bounds__(AW * 64) k_attn(const unsigned short* q, const unsigned short* kc, const unsigned short* vc, float* part) {
  __shared__ float mrg[AW * 8][DH + 4];
  const int bh = blockIdx.x / NSPLIT, s = blockIdx.x % NSPLIT;
  const int b = bh / H, h = bh % H;
  const int tid = threadIdx.x, lane = tid & 63, wave = __builtin_amdgcn_readfirstlane(tid >> 6), r = lane >> 3, j = lane & 7;
  u32x4 qf[3];
#pragma unroll
  for (int c = 0; c < 3; ++c) qf[c] = *reinterpret_cast<const u32x4*>(q + b * HD + h * DH + (j + 8 * c) * 8);
  const size_t base = ((size_t)bh * SK + (size_t)s * KPS) * DH;
  const unsigned short* kp = kc + base + (size_t)(wave * TT * 8 + r) * DH + j * 8;
  const unsigned short* vp = vc + base + (size_t)(wave * TT * 8 + r) * DH + j * 8;
  constexpr int NIT = KPS / (AW * TT * 8);
  constexpr size_t STEP = (size_t)AW * TT * 8 * DH;
  float m = -INFINITY, l = 0.f, acc[24];
#pragma unroll
  for (int i = 0; i < 24; ++i) acc[i] = 0.f;
  u32x4 Kn[TT][3], Vn[TT][3];
#define ISSUE(it) _Pragma("unroll") for (int t = 0; t < TT; ++t) _Pragma("unroll") for (int c = 0; c < 3; ++c) { \
    Kn[t][c] = __builtin_nontemporal_load(reinterpret_cast<const u32x4*>(kp + (it) * STEP + t * 8 * DH + c * 64)); } \
    _Pragma("unroll") for (int t = 0; t < TT; ++t) _Pragma("unroll") for (int c = 0; c < 3; ++c) { \
    Vn[t][c] = __builtin_nontemporal_load(reinterpret_cast<const u32x4*>(vp + (it) * STEP + t * 8 * DH + c * 64)); }
  ISSUE(0);
  for (int it = 0; it < NIT; ++it) {
    u32x4 Kc[TT][3], Vc[TT][3];
#pragma unroll
    for (int t = 0; t < TT; ++t)
#pragma unroll
      for (int c = 0; c < 3; ++c) { Kc[t][c] = Kn[t][c]; Vc[t][c] = Vn[t][c]; }
    if (it + 1 < NIT) { ISSUE(it + 1); }
    float sc[TT];
#pragma unroll
    for (int t = 0; t < TT; ++t) {
      float d = 0.f;
#pragma unroll
      for (int c = 0; c < 3; ++c)
#pragma unroll
        for (int k = 0; k < 4; ++k) d = dot2(Kc[t][c][k], qf[c][k], d);
      d += dpp<XOR1>(d); d += dpp<XOR2>(d); d += dpp<XOR7>(d);
      d = d * 0.07216878364870322f;
      sc[t] = d * 1.4426950408889634f;
    }
    float mn = m;
#pragma unroll
    for (int t = 0; t < TT; ++t) mn = fmaxf(mn, sc[t]);
    const float alpha = __builtin_amdgcn_exp2f(m - mn);
    m = mn;
    l *= alpha;
#pragma unroll
    for (int i = 0; i < 24; ++i) acc[i] *= alpha;
#pragma unroll
    for (int t = 0; t < TT; ++t) {
      const float p = __builtin_amdgcn_exp2f(sc[t] - mn);
      l += p;
#pragma unroll
      for (int c = 0; c < 3; ++c)
#pragma unroll
        for (int k = 0; k < 4; ++k) { acc[c * 8 + 2 * k] = fmaf(p, bflo(Vc[t][c][k]), acc[c * 8 + 2 * k]); acc[c * 8 + 2 * k + 1] = fmaf(p, bfhi(Vc[t][c][k]), acc[c * 8 + 2 * k + 1]); }
    }
  }
  float* rec = mrg[wave * 8 + r];
#pragma unroll
  for (int c = 0; c < 3; ++c)
#pragma unroll
    for (int k = 0; k < 8; ++k) rec[4 + (j + 8 * c) * 8 + k] = acc[c * 8 + k];
  if (j == 0) { rec[0] = m; rec[1] = l; }
  __syncthreads();
  if (tid < DH) {
    float M = -INFINITY;
    for (int g = 0; g < AW * 8; ++g) M = fmaxf(M, mrg[g][0]);
    float L = 0.f, o = 0.f;
    for (int g = 0; g < AW * 8; ++g) { const float w = __builtin_amdgcn_exp2f(mrg[g][0] - M); L = fmaf(w, mrg[g][1], L); o = fmaf(w, mrg[g][4 + tid], o); }
    float* dst = part + (size_t)blockIdx.x * 256;
    dst[4 + tid] = o;
    if (tid == 0) { dst[0] = M; dst[1] = L; }
  }
}

__global__ void __launch_bounds__(256) k_oproj(const float* part, const unsigned short* wo, float* hp) {
  __shared__ float os[4 * DH], red[32 * 256], res[256];
  const int t = threadIdx.x, h = blockIdx.y;
  for (int i = t; i < 4 * DH; i += 256) {
    const int b = i / DH, d = i % DH;
    const float* p0 = part + (size_t)((b * H + h) * NSPLIT) * 256;
    float M = -INFINITY;
    for (int s = 0; s < NSPLIT; ++s) M = fmaxf(M, p0[s * 256]);
    float L = 0.f, o = 0.f;
    for (int s = 0; s < NSPLIT; ++s) { const float w = __builtin_amdgcn_exp2f(p0[s * 256] - M); L = fmaf(w, p0[s * 256 + 1], L); o = fmaf(w, p0[s * 256 + 4 + d], o); }
    os[i] = rbf(o / L);
  }
  __syncthreads();
  gemv64<DH>(os, wo + (size_t)h * DH * D + blockIdx.x * 64, D, red, res);
  hp[((size_t)h * 4 + (t >> 6)) * D + blockIdx.x * 64 + (t & 63)] = res[t];
}

__global__ void __launch_bounds__(256) k_ffn1(const unsigned short* x, const float* hp, const unsigned short* wg, const unsigned short* wu, float* hbuf, float* act) {
  __shared__ float hs[4 * D], hn[4 * D], red[32 * 256], rg[256], ru[256];
  const int t = threadIdx.x;
  for (int i = t; i < 4 * D; i += 256) {
    float s = 0.f;
    for (int h = 0; h < H; ++h) s += hp[(size_t)h * 4 * D + i];
    const float hv = rbf(bf2f(x[i]) + rbf(s));
    hs[i] = hv;
    if (blockIdx.x == 0) hbuf[i] = hv;
  }
  __syncthreads();
  ln_row(hs + (t >> 6) * D, hn + (t >> 6) * D, t & 63);
  __syncthreads();
  gemv64<D>(hn, wg + blockIdx.x * 64, F, red, rg);
  gemv64<D>(hn, wu + blockIdx.x * 64, F, red, ru);
  const float g = rbf(rg[t]), u = rbf(ru[t]);
  const float sg = rbf(g / (1.f + __expf(-g)));
  act[(t >> 6) * F + blockIdx.x * 64 + (t & 63)] = rbf(sg * u);
}

__global__ void __launch_bounds__(256) k_ffn2(const float* hbuf, const float* act, const unsigned short* wd, unsigned short* out) {
  __shared__ float as[4 * F], red[32 * 256], res[256];
  const int t = threadIdx.x;
  for (int i = t; i < 4 * F; i += 256) as[i] = act[i];
  __syncthreads();
  gemv64<F>(as, wd + blockIdx.x * 64, D, red, res);
  const int o = (t >> 6) * D + blockIdx.x * 64 + (t & 63);
  out[o] = f2bf(hbuf[o] + rbf(res[t]));
}

extern "C" void kernel_launch(void* const* d_in, const int* in_sizes, int n_in, void* d_out, int out_size, void* d_ws, size_t ws_size, hipStream_t stream) {
  const unsigned short* x = (const unsigned short*)d_in[0];
  const unsigned short* kc = (const unsigned short*)d_in[1];
  const unsigned short* vc = (const unsigned short*)d_in[2];
  const unsigned short* wq = (const unsigned short*)d_in[3];
  const unsigned short* wo = (const unsigned short*)d_in[4];
  const unsigned short* wg = (const unsigned short*)d_in[6];
  const unsigned short* wu = (const unsigned short*)d_in[7];
  const unsigned short* wd = (const unsigned short*)d_in[8];
  char* ws = (char*)d_ws;
  unsigned short* q = (unsigned short*)ws;
  float* part = (float*)(ws + 65536);
  float* hp = (float*)(ws + 65536 + 262144);
  float* hbuf = (float*)(ws + 1048576);
  float* act = (float*)(ws + 1048576 + 65536);
  k_qproj<<<HD / 64, 256, 0, stream>>>(x, wq, q);
  k_attn<<<B * H * NSPLIT, AW * 64, 0, stream>>>(q, kc, vc, part);
  k_oproj<<<dim3(10, 32), 256, 0, stream>>>(part, wo, hp);
  k_ffn1<<<24, 256, 0, stream>>>(x, hp, wg, wu, hbuf, act);
  k_ffn2<<<10, 256, 0, stream>>>(hbuf, act, wd, (unsigned short*)d_out);
}
```

```cpp
#define LDK(p) __builtin_nontemporal_load(p)
#define LDV(p) __builtin_nontemporal_load(p)
#define LDW(p) (*(p))
#include <hip/hip_runtime.h>
#include <hip/hip_bf16.h>
#include <stdint.h>

#define MIMIC 1

typedef unsigned u32x4 __attribute__((ext_vector_type(4)));
typedef __bf16 bf16x2 __attribute__((ext_vector_type(2)));
typedef float f32x4 __attribute__((ext_vector_type(4)));

constexpr int B = 4, D = 640, H = 32, SK = 4096, DH = 192, F = 1536, HD = H * DH;
constexpr int NSPLIT = 2, KPS = SK / NSPLIT;

__device__ __forceinline__ float bf2f(unsigned short b) { return __builtin_bit_cast(float, (unsigned)b << 16); }
__device__ __forceinline__ float bflo(unsigned u) { return __builtin_bit_cast(float, u << 16); }
__device__ __forceinline__ float bfhi(unsigned u) { return __builtin_bit_cast(float, u & 0xffff0000u); }
__device__ __forceinline__ unsigned short f2bf(float f) { __bf16 b = (__bf16)f; return __builtin_bit_cast(unsigned short, b); }
__device__ __forceinline__ float rbf(float f) { return bf2f(f2bf(f)); }
__device__ __forceinline__ float dot2(unsigned k, unsigned q, float acc) {
  return __builtin_amdgcn_fdot2_f32_bf16(__builtin_bit_cast(bf16x2, k), __builtin_bit_cast(bf16x2, q), acc, false);
}
template <int CTRL> __device__ __forceinline__ float dpp(float x) {
  return __builtin_bit_cast(float, __builtin_amdgcn_mov_dpp(__builtin_bit_cast(int, x), CTRL, 0xf, 0xf, true));
}
constexpr int XOR1 = 0xB1, XOR2 = 0x4E, XOR7 = 0x141, XOR8 = 0x128;

__device__ __forceinline__ float wave_sum(float v) {
  for (int o = 32; o > 0; o >>= 1) v += __shfl_xor(v, o);
  return v;
}

__device__ __forceinline__ void ln_row(const float* in, float* out, int lane) {
  float v[10]; float s = 0.f;
#pragma unroll
  for (int i = 0; i < 10; ++i) { v[i] = in[lane + 64 * i]; s += v[i]; }
  s = wave_sum(s);
  const float mean = s * (1.f / D);
  float q = 0.f;
#pragma unroll
  for (int i = 0; i < 10; ++i) { const float d = v[i] - mean; q += d * d; }
  q = wave_sum(q);
  const float var = q * (1.f / D);
  const float mb = rbf(mean), vb = rbf(var);
  const float den = rbf(sqrtf(rbf(vb + 1e-5f)));
#pragma unroll
  for (int i = 0; i < 10; ++i) out[lane + 64 * i] = rbf(rbf(v[i] - mb) / den);
}

template <int KR>
__device__ __forceinline__ void gemv64(const float* xs  , const unsigned short* W, int ldw, float* red  , float* res  ) {
  const int t = threadIdx.x, c8 = t & 7, kg = t >> 3;
  float acc[4][8];
#pragma unroll
  for (int b = 0; b < 4; ++b)
#pragma unroll
    for (int j = 0; j < 8; ++j) acc[b][j] = 0.f;
#pragma unroll 4
  for (int k = kg; k < KR; k += 32) {
    const u32x4 w = LDW(reinterpret_cast<const u32x4*>(W + (size_t)k * ldw + c8 * 8));
#pragma unroll
    for (int b = 0; b < 4; ++b) {
      const float x = xs[b * KR + k];
#pragma unroll
      for (int j = 0; j < 4; ++j) { acc[b][2 * j] = fmaf(x, bflo(w[j]), acc[b][2 * j]); acc[b][2 * j + 1] = fmaf(x, bfhi(w[j]), acc[b][2 * j + 1]); }
    }
  }
#pragma unroll
  for (int b = 0; b < 4; ++b)
#pragma unroll
    for (int j = 0; j < 8; ++j) red[(kg * 4 + b) * 64 + c8 * 8 + j] = acc[b][j];
  __syncthreads();
  { float s = 0.f;
    for (int g = 0; g < 32; ++g) s += red[g * 256 + t];
    res[t] = s; }
  __syncthreads();
}

__global__ void __launch_bounds__(256) k_qproj(const unsigned short* x, const unsigned short* wq, unsigned short* q) {
  __shared__ float xs[4 * D], xn[4 * D], red[32 * 256], res[256];
  const int t = threadIdx.x;
  for (int i = t; i < 4 * D; i += 256) xs[i] = bf2f(x[i]);
  __syncthreads();
  ln_row(xs + (t >> 6) * D, xn + (t >> 6) * D, t & 63);
  __syncthreads();
  gemv64<D>(xn, wq + blockIdx.x * 64, HD, red, res);
  q[(t >> 6) * HD + blockIdx.x * 64 + (t & 63)] = f2bf(res[t]);
}

constexpr int AW = 8;
constexpr int TT = 2;
__global__ void __launch_bounds__(AW * 64) k_attn(const unsigned short* q, const unsigned short* kc, const unsigned short* vc, float* part) {
  __shared__ float mrg[AW * 8][DH + 4];
  const int bh = blockIdx.x / NSPLIT, s = blockIdx.x % NSPLIT;
  const int b = bh / H, h = bh % H;
  const int tid = threadIdx.x, lane = tid & 63, wave = __builtin_amdgcn_readfirstlane(tid >> 6), r = lane >> 3, j = lane & 7;
  u32x4 qf[3];
#pragma unroll
  for (int c = 0; c < 3; ++c) qf[c] = *reinterpret_cast<const u32x4*>(q + b * HD + h * DH + (j + 8 * c) * 8);
  const size_t base = ((size_t)bh * SK + (size_t)s * KPS) * DH;
  const unsigned short* kp = kc + base + (size_t)(wave * TT * 8 + r) * DH + j * 8;
  const unsigned short* vp = vc + base + (size_t)(wave * TT * 8 + r) * DH + j * 8;
  constexpr int NIT = KPS / (AW * TT * 8);
  constexpr size_t STEP = (size_t)AW * TT * 8 * DH;
  float m = -INFINITY, l = 0.f, acc[24];
#pragma unroll
  for (int i = 0; i < 24; ++i) acc[i] = 0.f;
  u32x4 Kn[TT][3], Vn[TT][3];
#define ISSUE(it) _Pragma("unroll") for (int t = 0; t < TT; ++t) _Pragma("unroll") for (int c = 0; c < 3; ++c) { \
    Kn[t][c] = LDK(reinterpret_cast<const u32x4*>(kp + (it) * STEP + t * 8 * DH + c * 64)); } \
    _Pragma("unroll") for (int t = 0; t < TT; ++t) _Pragma("unroll") for (int c = 0; c < 3; ++c) { \
    Vn[t][c] = LDV(reinterpret_cast<const u32x4*>(vp + (it) * STEP + t * 8 * DH + c * 64)); }
  ISSUE(0);
  for (int it = 0; it < NIT; ++it) {
    u32x4 Kc[TT][3], Vc[TT][3];
#pragma unroll
    for (int t = 0; t < TT; ++t)
#pragma unroll
      for (int c = 0; c < 3; ++c) { Kc[t][c] = Kn[t][c]; Vc[t][c] = Vn[t][c]; }
    if (it + 1 < NIT) { ISSUE(it + 1); }
    float sc[TT];
#pragma unroll
    for (int t = 0; t < TT; ++t) {
      float d = 0.f;
#pragma unroll
      for (int c = 0; c < 3; ++c)
#pragma unroll
        for (int k = 0; k < 4; ++k) d = dot2(Kc[t][c][k], qf[c][k], d);
      d += dpp<XOR1>(d); d += dpp<XOR2>(d); d += dpp<XOR7>(d);
      d = rbf(rbf(d) * 0.07216878364870322f);
      sc[t] = d * 1.4426950408889634f;
    }
    float mn = m;
#pragma unroll
    for (int t = 0; t < TT; ++t) mn = fmaxf(mn, sc[t]);
    const float alpha = __builtin_amdgcn_exp2f(m - mn);
    m = mn;
    l *= alpha;
#pragma unroll
    for (int i = 0; i < 24; ++i) acc[i] *= alpha;
#pragma unroll
    for (int t = 0; t < TT; ++t) {
      const float p = __builtin_amdgcn_exp2f(sc[t] - mn);
      l += p;
#pragma unroll
      for (int c = 0; c < 3; ++c)
#pragma unroll
        for (int k = 0; k < 4; ++k) { acc[c * 8 + 2 * k] = fmaf(p, bflo(Vc[t][c][k]), acc[c * 8 + 2 * k]); acc[c * 8 + 2 * k + 1] = fmaf(p, bfhi(Vc[t][c][k]), acc[c * 8 + 2 * k + 1]); }
    }
  }
  float* rec = mrg[wave * 8 + r];
#pragma unroll
  for (int c = 0; c < 3; ++c)
#pragma unroll
    for (int k = 0; k < 8; ++k) rec[4 + (j + 8 * c) * 8 + k] = acc[c * 8 + k];
  if (j == 0) { rec[0] = m; rec[1] = l; }
  __syncthreads();
  if (tid < DH) {
    float M = -INFINITY;
    for (int g = 0; g < AW * 8; ++g) M = fmaxf(M, mrg[g][0]);
    float L = 0.f, o = 0.f;
    for (int g = 0; g < AW * 8; ++g) { const float w = __builtin_amdgcn_exp2f(mrg[g][0] - M); L = fmaf(w, mrg[g][1], L); o = fmaf(w, mrg[g][4 + tid], o); }
    float* dst = part + (size_t)blockIdx.x * 256;
    dst[4 + tid] = o;
    if (tid == 0) { dst[0] = M; dst[1] = L; }
  }
}

__global__ void __launch_bounds__(256) k_oproj(const float* part, const unsigned short* wo, float* hp) {
  __shared__ float os[4 * DH], red[32 * 256], res[256];
  const int t = threadIdx.x, h = blockIdx.y;
  for (int i = t; i < 4 * DH; i += 256) {
    const int b = i / DH, d = i % DH;
    const float* p0 = part + (size_t)((b * H + h) * NSPLIT) * 256;
    float M = -INFINITY;
    for (int s = 0; s < NSPLIT; ++s) M = fmaxf(M, p0[s * 256]);
    float L = 0.f, o = 0.f;
    for (int s = 0; s < NSPLIT; ++s) { const float w = __builtin_amdgcn_exp2f(p0[s * 256] - M); L = fmaf(w, p0[s * 256 + 1], L); o = fmaf(w, p0[s * 256 + 4 + d], o); }
    os[i] = rbf(o / L);
  }
  __syncthreads();
  gemv64<DH>(os, wo + (size_t)h * DH * D + blockIdx.x * 64, D, red, res);
  hp[((size_t)h * 4 + (t >> 6)) * D + blockIdx.x * 64 + (t & 63)] = res[t];
}

__global__ void __launch_bounds__(256) k_ffn1(const unsigned short* x, const float* hp, const unsigned short* wg, const unsigned short* wu, float* hbuf, float* act) {
  __shared__ float hs[4 * D], hn[4 * D], red[32 * 256], rg[256], ru[256];
  const int t = threadIdx.x;
  for (int i = t; i < 4 * D; i += 256) {
    float s = 0.f;
    for (int h = 0; h < H; ++h) s += hp[(size_t)h * 4 * D + i];
    const float hv = rbf(bf2f(x[i]) + rbf(s));
    hs[i] = hv;
    if (blockIdx.x == 0) hbuf[i] = hv;
  }
  __syncthreads();
  ln_row(hs + (t >> 6) * D, hn + (t >> 6) * D, t & 63);
  __syncthreads();
  gemv64<D>(hn, wg + blockIdx.x * 64, F, red, rg);
  gemv64<D>(hn, wu + blockIdx.x * 64, F, red, ru);
  const float g = rbf(rg[t]), u = rbf(ru[t]);
  const float sg = rbf(g / (1.f + __expf(-g)));
  act[(t >> 6) * F + blockIdx.x * 64 + (t & 63)] = rbf(sg * u);
}

__global__ void __launch_bounds__(256) k_ffn2(const float* hbuf, const float* act, const unsigned short* wd, unsigned short* out) {
  __shared__ float as[4 * F], red[32 * 256], res[256];
  const int t = threadIdx.x;
  for (int i = t; i < 4 * F; i += 256) as[i] = act[i];
  __syncthreads();
  gemv64<F>(as, wd + blockIdx.x * 64, D, red, res);
  const int o = (t >> 6) * D + blockIdx.x * 64 + (t & 63);
  out[o] = f2bf(hbuf[o] + rbf(res[t]));
}

extern "C" void kernel_launch(void* const* d_in, const int* in_sizes, int n_in, void* d_out, int out_size, void* d_ws, size_t ws_size, hipStream_t stream) {
  const unsigned short* x = (const unsigned short*)d_in[0];
  const unsigned short* kc = (const unsigned short*)d_in[1];
  const unsigned short* vc = (const unsigned short*)d_in[2];
  const unsigned short* wq = (const unsigned short*)d_in[3];
  const unsigned short* wo = (const unsigned short*)d_in[4];
  const unsigned short* wg = (const unsigned short*)d_in[6];
  const unsigned short* wu = (const unsigned short*)d_in[7];
  const unsigned short* wd = (const unsigned short*)d_in[8];
  char* ws = (char*)d_ws;
  unsigned short* q = (unsigned short*)ws;
  float* part = (float*)(ws + 65536);
  float* hp = (float*)(ws + 65536 + 262144);
  float* hbuf = (float*)(ws + 1048576);
  float* act = (float*)(ws + 1048576 + 65536);
  k_qproj<<<HD / 64, 256, 0, stream>>>(x, wq, q);
  k_attn<<<B * H * NSPLIT, AW * 64, 0, stream>>>(q, kc, vc, part);
  k_oproj<<<dim3(10, 32), 256, 0, stream>>>(part, wo, hp);
  k_ffn1<<<24, 256, 0, stream>>>(x, hp, wg, wu, hbuf, act);
  k_ffn2<<<10, 256, 0, stream>>>(hbuf, act, wd, (unsigned short*)d_out);
}
```

```cpp
#include <hip/hip_runtime.h>
#include <stdint.h>

#define GAS __attribute__((address_space(1)))
#define LAS __attribute__((address_space(3)))
typedef unsigned u32;
typedef unsigned long long u64;
typedef unsigned short u16;
typedef unsigned u32x4 __attribute__((ext_vector_type(4)));
typedef unsigned u32x2 __attribute__((ext_vector_type(2)));
typedef int i32x4 __attribute__((ext_vector_type(4)));
typedef float f32x4 __attribute__((ext_vector_type(4)));
typedef float f32x2 __attribute__((ext_vector_type(2)));
typedef __bf16 bf16x2 __attribute__((ext_vector_type(2)));
typedef __bf16 bf16x8 __attribute__((ext_vector_type(8)));
typedef short s16x4 __attribute__((ext_vector_type(4)));
#define RLX_AGENT __ATOMIC_RELAXED, __HIP_MEMORY_SCOPE_AGENT
#define RLX_WG __ATOMIC_RELAXED, __HIP_MEMORY_SCOPE_WORKGROUP
#define RFL(x) __builtin_amdgcn_readfirstlane(x)

constexpr int D = 640, H = 32, SK = 4096, DH = 192, F = 1536, HD = H * DH;
constexpr int LAG = 3;
constexpr int LOOK = 10;
constexpr int RH = 4, RA = 4;
constexpr int NSP = 96, UD = 2 * NSP, NSD = 16, NDYN = 256 - 8 * NSD;
constexpr int NSLOT0 = 24, NSW = 48, RS = 10;
static_assert(LAG >= 2 && LAG <= 5, "");
constexpr u32 REC_SZ = 1664;
constexpr u32 HG_SZ = 40 * 256, AG_SZ = 96 * 256;
constexpr u32 WS_EPOCH = 0, WS_TICK = 4096, WS_QG = 16384, WS_HG = WS_QG + 192 * 2048, WS_AG = WS_HG + RH * HG_SZ,
              WS_REC3 = WS_AG + RA * AG_SZ, WS_PG = WS_REC3 + 256 * REC_SZ, WS_REC0 = WS_PG + 40 * 32 * 512,
              WS_END = 4u << 20;
constexpr u32 ER_SZ = 1408;
static_assert(WS_REC0 + 256 * ER_SZ <= (2u << 20) && RH <= 4 && RA <= 4, "ws map");
constexpr u32 OOB = 0xFFFFFF00u;
constexpr u32 L_WO = RS * 6144, L_WR = L_WO + 30720;
constexpr u32 L_CTRL = NSLOT0 * 6144;
constexpr u32 L_QS = L_CTRL + 256;
constexpr u32 L_OS = L_QS + 1536;
constexpr u32 L_Z = L_OS + 1792;
constexpr u32 L_XN = L_Z, L_QP = L_Z + 5120, L_WREC = L_Z, L_END = L_Z + 16 * 776;
static_assert(L_END <= 163840 && L_QP + 4096 <= L_END && L_WR + 49152 <= L_CTRL, "lds map");
constexpr u32 L_RED = 0, L_HN = 4096, L_AS = 16384;
enum { C_QREADY = 0, C_QCNT = 1, C_FCNT = 2  , C_XCNT = 8, C_TAG = 9, C_FULL = 12, C_END = 13  ,
       C_PROG = 16  , C_FIFO = 32   };

__shared__ __attribute__((aligned(16))) char smem[163840];

__device__ __forceinline__ float asf(u32 u) { return __builtin_bit_cast(float, u); }
__device__ __forceinline__ u32 asu(float f) { return __builtin_bit_cast(u32, f); }
__device__ __forceinline__ float bflo(u32 u) { return asf(u << 16); }
__device__ __forceinline__ float bfhi(u32 u) { return asf(u & 0xffff0000u); }
__device__ __forceinline__ u32 cvtpk(float lo, float hi) { const bf16x2 r = {(__bf16)lo, (__bf16)hi}; return __builtin_bit_cast(u32, r); }
typedef _Float16 f16x2 __attribute__((ext_vector_type(2)));
__device__ __forceinline__ u32 cvth(float lo, float hi) { const f16x2 r = {(_Float16)lo, (_Float16)hi}; return __builtin_bit_cast(u32, r); }
__device__ __forceinline__ float rbf(float f) { return bflo(cvtpk(f, f)); }
template <int CTRL> __device__ __forceinline__ u32 dppu(u32 x) { return (u32)__builtin_amdgcn_mov_dpp((int)x, CTRL, 0xf, 0xf, true); }
template <int CTRL> __device__ __forceinline__ float dpp(float x) { return asf(dppu<CTRL>(asu(x))); }
constexpr int XOR1 = 0xB1, XOR2 = 0x4E, HMIR = 0x141, ROR8 = 0x128, ROR4 = 0x124, ROR2 = 0x122, ROR1 = 0x121, QP0 = 0x00, QP1 = 0x55, QP2 = 0xAA, QP3 = 0xFF;
__device__ __forceinline__ float sw16_add(float a, float b) {
  auto s = __builtin_amdgcn_permlane16_swap(asu(a), asu(b), false, false); return asf(s[0]) + asf(s[1]);
}
__device__ __forceinline__ float sw32_add(float a, float b) {
  auto s = __builtin_amdgcn_permlane32_swap(asu(a), asu(b), false, false); return asf(s[0]) + asf(s[1]);
}
__device__ __forceinline__ float sw16_max(float a) { auto s = __builtin_amdgcn_permlane16_swap(asu(a), asu(a), false, false); return fmaxf(asf(s[0]), asf(s[1])); }
__device__ __forceinline__ float sw32_max(float a) { auto s = __builtin_amdgcn_permlane32_swap(asu(a), asu(a), false, false); return fmaxf(asf(s[0]), asf(s[1])); }
__device__ __forceinline__ float ex2(float x) { return __builtin_amdgcn_exp2f(x); }
template <typename T> __device__ __forceinline__ T lds_ld(u32 off) { return *(const LAS T*)((LAS char*)smem + off); }
template <typename T> __device__ __forceinline__ void lds_st(u32 off, T v) { *(LAS T*)((LAS char*)smem + off) = v; }
__device__ __forceinline__ u32 ctl_ld(int i) { return RFL(__hip_atomic_load((LAS u32*)((LAS char*)smem + L_CTRL) + i, RLX_WG)); }
__device__ __forceinline__ void ctl_st(int i, u32 v) { __hip_atomic_store((LAS u32*)((LAS char*)smem + L_CTRL) + i, v, RLX_WG); }
#define VM0() asm volatile("s_waitcnt vmcnt(0)" ::: "memory")
#define BAR() asm volatile("s_waitcnt lgkmcnt(0)\n\ts_barrier" ::: "memory")
__device__ __forceinline__ u32 ctl_arrive(int i, int lane) {
  u32 old; u64 ex;
  const u32 a = L_CTRL + i * 4, one = 1u;
  asm volatile("s_mov_b64 %1, exec\n\ts_mov_b64 exec, 1\n\tds_add_rtn_u32 %0, %2, %3\n\ts_mov_b64 exec, %1\n\ts_waitcnt lgkmcnt(0)"
               : "=&v"(old), "=&s"(ex) : "v"(a), "v"(one) : "memory");
  return RFL(old);
}
constexpr u32 CAP_LDS = 1u << 21, CAP_G = 1u << 16;
template <int SLP> __device__ __forceinline__ void ctl_wait_ge(int i, int want) {
#pragma unroll 1
  for (u32 n = 0; (int)ctl_ld(i) < want && n < CAP_LDS; ++n) __builtin_amdgcn_s_sleep(SLP);
  asm volatile("" ::: "memory");
}
__device__ __forceinline__ void wait_pos(int want) {
#pragma unroll 1
  for (u32 n = 0; (int)ctl_ld(C_FULL) < want && (int)ctl_ld(C_END) == 0x7fffffff && n < CAP_LDS; ++n) __builtin_amdgcn_s_sleep(8);
}
__device__ __forceinline__ u32 get_tag() {
  u32 t = ctl_ld(C_TAG);
#pragma unroll 1
  for (u32 n = 0; t == 0u && n < CAP_LDS; ++n) { __builtin_amdgcn_s_sleep(1); t = ctl_ld(C_TAG); }
  return t;
}
__device__ __forceinline__ i32x4 mkrsrc(const void* p, u32 bytes) {
  const u64 a = (u64)p; return i32x4{(int)(u32)a, (int)((u32)(a >> 32) & 0xffffu), (int)bytes, 0x00020000};
}
#define DMA1(voff, rs, ldsa, soff) asm volatile("s_mov_b32 m0, %2\n\ts_nop 3\n\tbuffer_load_dwordx4 %0, %1, %3 offen lds" \
    :: "v"(voff), "s"(rs), "s"(RFL(ldsa)), "s"(RFL(soff)) : "memory")

typedef __amdgpu_buffer_rsrc_t rsrc_t;
__device__ __forceinline__ void put(rsrc_t rs, u32 off, u32 tag, u32 a, u32 b) { __builtin_amdgcn_raw_buffer_store_b128(u32x4{a, tag, b, tag}, rs, off, 0, 16); }
template <int N, int SLP>
__device__ __forceinline__ void sweep(rsrc_t rs, u32 (&o)[N], u32 tag, u32 (&a)[N], u32 (&b)[N]) {
#pragma unroll
  for (int k = 0; k < N; ++k) { a[k] = 0; b[k] = 0; }
#pragma unroll 1
  for (u32 n = 0; n < CAP_G; ++n) {
    asm volatile("" ::: "memory");
    u32x4 x[N];
#pragma unroll
    for (int k = 0; k < N; ++k) x[k] = __builtin_amdgcn_raw_buffer_load_b128(rs, o[k], 0, 16);
    u32 all = 0x80000000u;
#pragma unroll
    for (int k = 0; k < N; ++k) {
      const bool hit = x[k][1] == tag && x[k][3] == tag;
      a[k] = hit ? x[k][0] : a[k]; b[k] = hit ? x[k][2] : b[k];
      o[k] = hit ? OOB : o[k];
      all &= o[k];
    }
    if (__all(all != 0u)) break;
    if (SLP) __builtin_amdgcn_s_sleep(SLP);
  }
  asm volatile("" ::: "memory");
}

template <int N>
__device__ __forceinline__ void lnorm(float (&v)[N], bool tl) {
  const float pv = asf(RFL(asu(v[0])));
  float d[N];
#pragma unroll
  for (int i = 0; i < N; ++i) d[i] = (i < 8 || tl) ? v[i] - pv : 0.f;
  float s = ((d[0] + d[1]) + (d[2] + d[3])) + ((d[4] + d[5]) + (d[6] + d[7]));
  float q = (fmaf(d[1], d[1], d[0] * d[0]) + fmaf(d[3], d[3], d[2] * d[2])) + (fmaf(d[5], d[5], d[4] * d[4]) + fmaf(d[7], d[7], d[6] * d[6]));
#pragma unroll
  for (int i = 8; i < N; i += 2) { s += d[i] + d[i + 1]; q += fmaf(d[i + 1], d[i + 1], d[i] * d[i]); }
  s += dpp<ROR8>(s); q += dpp<ROR8>(q); s += dpp<ROR4>(s); q += dpp<ROR4>(q); s += dpp<ROR2>(s); q += dpp<ROR2>(q); s += dpp<ROR1>(s); q += dpp<ROR1>(q);
  s = sw16_add(s, s); q = sw16_add(q, q); s = sw32_add(s, s); q = sw32_add(q, q);
  const float md = s * (1.f / D), mean = pv + md, var = fmaxf(fmaf(-md, md, q * (1.f / D)), 0.f);
  const float mb = rbf(mean);
  const float den = rbf(__builtin_amdgcn_sqrtf(rbf(rbf(var) + 1e-5f)));
  const float rc = __builtin_amdgcn_rcpf(den);
#pragma unroll
  for (int i = 0; i < N; ++i) { const float a = rbf(v[i] - mb), q0 = a * rc; v[i] = fmaf(fmaf(-q0, den, a), rc, q0); }
}

template <int SLP>
__device__ __forceinline__ u32 combine(rsrc_t rs, u32 rec, int k, int lane, u32 tag) {
  u32 o[5], a[5], b[5];
#pragma unroll
  for (int i = 0; i < 4; ++i) o[i] = rec + (2 * i + (lane >> 5)) * REC_SZ + (32 * k + (lane & 31)) * 16;
  o[4] = rec + (lane & 7) * REC_SZ + 96 * 16;
  sweep<5, SLP>(rs, o, tag, a, b);
  const float m = asf(a[4]);
  float M = fmaxf(m, dpp<XOR1>(m)); M = fmaxf(M, dpp<XOR2>(M)); M = fmaxf(M, dpp<HMIR>(M));
  const float w = ex2(m - M);
  float L = w * asf(b[4]);
  L += dpp<XOR1>(L); L += dpp<XOR2>(L); L += dpp<HMIR>(L);
  float o0 = 0.f, o1 = 0.f;
#pragma unroll
  for (int i = 0; i < 4; ++i) {
    const float w0 = asf(__builtin_amdgcn_readlane(asu(w), 2 * i)), w1 = asf(__builtin_amdgcn_readlane(asu(w), 2 * i + 1)), wi = lane >= 32 ? w1 : w0;
    o0 = fmaf(wi, asf(a[i]), o0); o1 = fmaf(wi, asf(b[i]), o1);
  }
  const float rl = __builtin_amdgcn_rcpf(L);
  return cvtpk(sw32_add(o0, o0) * rl, sw32_add(o1, o1) * rl);
}

__device__ __forceinline__ void publish_early(rsrc_t rs, int b, int wg, int lane) {
  const u32 w0 = L_WREC + b * 4 * 776, c = lane < 24 ? lane : 0;
  f32x2 hd[4]; f32x4 r0[4], r1[4];
#pragma unroll
  for (int w = 0; w < 4; ++w) { hd[w] = lds_ld<f32x2>(w0 + w * 776 + 768);
    const f32x2 a = lds_ld<f32x2>(w0 + w * 776 + c * 32), a2 = lds_ld<f32x2>(w0 + w * 776 + c * 32 + 8), a3 = lds_ld<f32x2>(w0 + w * 776 + c * 32 + 16), a4 = lds_ld<f32x2>(w0 + w * 776 + c * 32 + 24);
    r0[w] = f32x4{a[0], a[1], a2[0], a2[1]}; r1[w] = f32x4{a3[0], a3[1], a4[0], a4[1]}; }
  const float MM = fmaxf(fmaxf(hd[0][0], hd[1][0]), fmaxf(hd[2][0], hd[3][0]));
  float LL = 0.f; f32x4 x0 = {0.f, 0.f, 0.f, 0.f}, x1 = {0.f, 0.f, 0.f, 0.f};
#pragma unroll
  for (int w = 0; w < 4; ++w) { const float e = ex2(hd[w][0] - MM); LL = fmaf(e, hd[w][1], LL); x0 += r0[w] * e; x1 += r1[w] * e; }
  float am = fmaxf(fmaxf(fmaxf(fabsf(x0[0]), fabsf(x0[1])), fmaxf(fabsf(x0[2]), fabsf(x0[3]))), fmaxf(fmaxf(fabsf(x1[0]), fabsf(x1[1])), fmaxf(fabsf(x1[2]), fabsf(x1[3]))));
  am = fmaxf(am, dpp<ROR8>(am)); am = fmaxf(am, dpp<ROR4>(am)); am = fmaxf(am, dpp<ROR2>(am)); am = fmaxf(am, dpp<ROR1>(am)); am = sw16_max(am);
  u32 eb = asu(am) & 0x7f800000u;
  eb = eb < 0x01000000u ? 0x01000000u : eb > 0x7e000000u ? 0x7e000000u : eb;
  const float rsc = asf(0x7f000000u - eb);
  x0 *= rsc; x1 *= rsc;
  u32x4 pk = {cvth(x0[0], x0[1]), cvth(x0[2], x0[3]), cvth(x1[0], x1[1]), cvth(x1[2], x1[3])};
  if (lane == 24) pk = u32x4{asu(MM), asu(LL), eb, 0u};
  __builtin_amdgcn_raw_buffer_store_b128(pk, rs, lane <= 24 ? WS_REC0 + wg * ER_SZ + b * 400 + lane * 16 : OOB, 0, 16);
}
__device__ __forceinline__ void gather_early(rsrc_t rs, int hp, int lane) {
  const u32 rec = WS_REC0 + (hp * 8 + (lane & 7)) * ER_SZ;
  u32x4 v[9], hd[3];
#pragma unroll
  for (int b = 0; b < 3; ++b) {
#pragma unroll
    for (int i = 0; i < 3; ++i) v[3 * b + i] = __builtin_amdgcn_raw_buffer_load_b128(rs, rec + b * 400 + ((lane >> 3) + 8 * i) * 16, 0, 16);
    hd[b] = __builtin_amdgcn_raw_buffer_load_b128(rs, rec + b * 400 + 384, 0, 16);
  }
#pragma unroll
  for (int b = 0; b < 3; ++b) {
    const float m = asf(hd[b][0]);
    float M = fmaxf(m, dpp<XOR1>(m)); M = fmaxf(M, dpp<XOR2>(M)); M = fmaxf(M, dpp<HMIR>(M));
    float w = ex2(m - M), L = w * asf(hd[b][1]);
    L += dpp<XOR1>(L); L += dpp<XOR2>(L); L += dpp<HMIR>(L);
    w *= __builtin_amdgcn_rcpf(L);
    const float sc = asf(hd[b][2]);
#pragma unroll
    for (int i = 0; i < 3; ++i) {
      float x[8];
#pragma unroll
      for (int e = 0; e < 4; ++e) { const u32 u = v[3 * b + i][e]; const f16x2 h = __builtin_bit_cast(f16x2, u); x[2 * e] = (w * (float)h[0]) * sc; x[2 * e + 1] = (w * (float)h[1]) * sc; }
#pragma unroll
      for (int e = 0; e < 4; ++e) { const float t1 = x[e] + dpp<HMIR>(x[e]), t2 = x[e + 4] + dpp<HMIR>(x[e + 4]); x[e] = (lane & 4) ? t2 : t1; }
#pragma unroll
      for (int e = 0; e < 2; ++e) { const float t1 = x[e] + dpp<XOR2>(x[e]), t2 = x[e + 2] + dpp<XOR2>(x[e + 2]); x[e] = (lane & 2) ? t2 : t1; }
      { const float t1 = x[0] + dpp<XOR1>(x[0]), t2 = x[1] + dpp<XOR1>(x[1]); x[0] = (lane & 1) ? t2 : t1; }
      lds_st<u16>(L_OS + b * 448 + i * 128 + lane * 2, (u16)cvtpk(x[0], x[0]));
    }
  }
}

__device__ __forceinline__ void publish_rec(rsrc_t rs, int wg, int lane, u32 tag) {
  const u32 w0 = L_WREC + 3 * 4 * 776;
  f32x2 hd[4], r0[4], r1[4];
#pragma unroll
  for (int w = 0; w < 4; ++w) { hd[w] = lds_ld<f32x2>(w0 + w * 776 + 768); r0[w] = lds_ld<f32x2>(w0 + w * 776 + lane * 8); r1[w] = lds_ld<f32x2>(w0 + w * 776 + 512 + (lane & 31) * 8); }
  const float MM = fmaxf(fmaxf(hd[0][0], hd[1][0]), fmaxf(hd[2][0], hd[3][0]));
  float LL = 0.f; f32x2 x0 = {0.f, 0.f}, x1 = {0.f, 0.f};
#pragma unroll
  for (int w = 0; w < 4; ++w) { const float e = ex2(hd[w][0] - MM); LL = fmaf(e, hd[w][1], LL); x0 += r0[w] * e; x1 += r1[w] * e; }
  const u32 rec = WS_REC3 + wg * REC_SZ;
  put(rs, rec + lane * 16, tag, asu(x0[0]), asu(x0[1]));
  put(rs, lane <= 32 ? rec + 1024 + lane * 16 : OOB, tag, asu(lane == 32 ? MM : x1[0]), asu(lane == 32 ? LL : x1[1]));
}

__device__ __forceinline__ u32x4 trfrag(u32 a, u32 hi) {
  const u32x2 x = __builtin_bit_cast(u32x2, __builtin_amdgcn_ds_read_tr16_b64_v4i16((LAS s16x4*)((LAS char*)smem + a)));
  const u32x2 y = __builtin_bit_cast(u32x2, __builtin_amdgcn_ds_read_tr16_b64_v4i16((LAS s16x4*)((LAS char*)smem + a + hi)));
  return u32x4{x[0], x[1], y[0], y[1]};
}
#define MFMA32(a_, b_, c_) __builtin_amdgcn_mfma_f32_16x16x32_bf16(__builtin_bit_cast(bf16x8, a_), __builtin_bit_cast(bf16x8, b_), c_, 0, 0, 0)
__device__ __forceinline__ float pick4(f32x4 c, int g4) { const float x = (g4 & 1) ? c[1] : c[0], y = (g4 & 1) ? c[3] : c[2]; return (g4 & 2) ? y : x; }
__device__ __forceinline__ void put_row4(rsrc_t rs, u32 base, u32 rep_sz, int nrep, int lane, u32 tag, float v) {
  const u32 lo = cvtpk(dpp<QP0>(v), dpp<QP1>(v)), hi = cvtpk(dpp<QP2>(v), dpp<QP3>(v));
  put(rs, (lane & 3) < nrep ? base + (lane & 3) * rep_sz + (lane >> 2) * 16 : OOB, tag, lo, hi);
}

__global__ void __launch_bounds__(512, 2)
fused_decode(const u16* __restrict__ xin, const u16* __restrict__ kc, const u16* __restrict__ vc, const u16* __restrict__ wq,
             const u16* __restrict__ wo, const u16* __restrict__ wgate, const u16* __restrict__ wup, const u16* __restrict__ wdown,
             u16* __restrict__ out, char* ws) {
  const int tid = threadIdx.x, lane = tid & 63, wave = RFL(tid >> 6);
  const int xx = blockIdx.x & 7, g = blockIdx.x >> 3;
  const int hp = xx * 4 + (g >> 3), cs = g & 7;
  const int gr = (g + 4 * xx) & 31;
  const int pid = xx * 24 + gr - 8;
  const bool hasq = gr >= 8, role_c = gr < 12, role_d = gr >= 12 && gr < 17;
  const int ri = xx * 5 + gr - 12;
  asm volatile("" :: "s"(xin), "s"(kc), "s"(vc), "s"(wq), "s"(wo), "s"(wgate), "s"(wup), "s"(wdown), "s"(out), "s"(ws));
  const u32 voff = lane * 16;
  const int g4 = lane >> 4;
  const rsrc_t rs = __builtin_amdgcn_make_buffer_rsrc((void*)ws, 0, WS_END, 0x00020000);
  u32 tag = 0;

  if (__builtin_expect(wave == 4 || wave == 5, 1)) {
    constexpr u32 SPAN = 3u * H * SK * DH * 2 + SK * DH * 2;
    const i32x4 rk = mkrsrc(kc + (size_t)g * SK * DH, SPAN), rv = mkrsrc(vc + (size_t)g * SK * DH, SPAN);
    const int par = wave & 1;
    u32 vk0[3], vk1[3], vv[3];
#pragma unroll
    for (int j = 0; j < 3; ++j) { const u32 r = ((j * 64 + lane) * 43691u) >> 20, hi = (lane & ~7) * 16;
      vk0[j] = hi + (((lane & 7) ^ (r >> 1)) * 16); vk1[j] = vk0[j] ^ 64; vv[j] = hi + (((lane & 7) ^ (r & 6)) * 16); }
#define STILE(i_) (u32)((((i_) >> 3) << 6) + (xx << 3) + ((i_) & 7))
#define TOFF(t_) (((t_) >> 9) * (u32)(H * SK * DH * 2) + (((t_) + g * 104) & 511) * 3072u)
#define UNIT(rs_, a_, b_, sl_, t0_, t1_) do { const u32 so_ = TOFF(t0_), s1_ = TOFF(t1_); \
    asm volatile("s_mov_b32 m0, %1\n\ts_nop 3\n\tbuffer_load_dwordx4 %4, %0, %2 offen nt lds\n\tbuffer_load_dwordx4 %5, %0, %2 offen offset:1024 nt lds\n\t" \
      "buffer_load_dwordx4 %6, %0, %2 offen offset:2048 nt lds\n\ts_add_u32 m0, m0, 0xc00\n\ts_nop 0\n\tbuffer_load_dwordx4 %7, %0, %3 offen nt lds\n\t" \
      "buffer_load_dwordx4 %8, %0, %3 offen offset:1024 nt lds\n\tbuffer_load_dwordx4 %9, %0, %3 offen offset:2048 nt lds" \
      :: "s"(rs_), "s"(RFL(sl_)), "s"(RFL(so_)), "s"(RFL(s1_)), "v"(a_[0]), "v"(a_[1]), "v"(a_[2]), "v"(b_[0]), "v"(b_[1]), "v"(b_[2]) : "memory", "scc"); } while (0)
    __builtin_amdgcn_s_setprio(3);
#pragma unroll 1
    for (int j = 0; j < LAG - 1; ++j) UNIT(rk, vk0, vk1, (2 * j + par) * 6144, STILE(4 * j + 2 * par), STILE(4 * j + 2 * par + 1));
    BAR();
    int i = 2 * (LAG - 1) + par;
    u32 sl = i * 6144;
    const LAS u32* ctl = (const LAS u32*)((LAS char*)smem + L_CTRL);
    u32 pv = 0, en = 0;
#define NEED(i_) ((i_) < NSW + RS ? (i_) - NSLOT0 : (i_) - RS)
#define OWNER(n_) ((n_) < UD ? (n_) & 3 : (((n_) - UD) >> 1) & 3)
#pragma unroll 1
    for (;; i += 2) {
      const int sp = 2 * (i < NSP ? i : i < UD ? i - NSP : NSP + ((i - UD) >> 1));
      u32 t0 = STILE(sp), t1 = STILE(sp + 1);
      if (i >= UD + 2 * NSD) {
        const u32 j = (i - UD) >> 1;
        u32 e1 = RFL(en);
#pragma unroll 1
        for (u32 n = 0; (e1 >> 16) != j && n < CAP_LDS; ++n) e1 = ctl_ld(C_FIFO + (j & 31));
        const u32 gp = e1 & 0xffffu;
        if (gp >= 1024u || (e1 >> 16) != j) break;
        t0 = 2 * gp; t1 = t0 + 1;
      }
      if (i >= NSLOT0) {
        const int need = NEED(i), cnt = need < UD ? need >> 2 : NSP / 2 + ((need - UD) >> 3);
        u32 p1 = RFL(pv);
#pragma unroll 1
        for (u32 n = 0; (int)p1 <= cnt && n < CAP_LDS; ++n) p1 = ctl_ld(C_PROG + OWNER(need));
      }
      if (i < NSP || (i >= UD && !par)) UNIT(rk, vk0, vk1, sl, t0, t1); else UNIT(rv, vv, vv, sl, t0, t1);
      { const u32 lim = (i < NSW ? NSLOT0 : RS) * 6144; sl += 12288; if (sl >= lim) sl -= lim; }
      { const int nn = NEED(i + 2);
        pv = __hip_atomic_load(ctl + C_PROG + OWNER(nn), RLX_WG); en = __hip_atomic_load(ctl + C_FIFO + (((i + 2 - UD) >> 1) & 31), RLX_WG); }
      asm volatile("s_waitcnt vmcnt(%0)" :: "n"(6 * (LAG - 1)) : "memory");
      ctl_st(C_FULL + 2 * par, (u32)(i + 1 - 2 * (LAG - 1)));
    }
    ctl_st(C_END + 2 * par, (u32)i);
#pragma unroll
    for (int r = LAG - 2; r >= 0; --r) { asm volatile("s_waitcnt vmcnt(%0)" :: "n"(6 * r) : "memory"); ctl_st(C_FULL + 2 * par, (u32)(i - 1 - 2 * r)); }
    __builtin_amdgcn_s_setprio(0);
    tag = get_tag();
  } else if (__builtin_expect(wave < 4, 1)) {
    u32 xr[5];
    u32x4 wv[10];
#pragma unroll
    for (int i = 0; i < 5; ++i) xr[i] = ((const GAS u32*)xin)[wave * 320 + i * 64 + lane];
    const rsrc_t rq = __builtin_amdgcn_make_buffer_rsrc((void*)wq, 0, hasq ? D * HD * 2 : 0, 0x00020000);
#pragma unroll
    for (int i = 0; i < 10; ++i)
      wv[i] = __builtin_amdgcn_raw_buffer_load_b128(rq, (lane >> 3) * (HD * 2) + (lane & 7) * 16, ((pid & 1) * 320 + 80 * wave + 8 * i) * (HD * 2) + (pid >> 1) * 128, 2);
    BAR();
    {
      float xn[10];
#pragma unroll
      for (int i = 0; i < 5; ++i) { xn[2 * i] = bflo(xr[i]); xn[2 * i + 1] = bfhi(xr[i]); }
      lnorm<10>(xn, true);
#pragma unroll
      for (int i = 0; i < 10; ++i) { const u32 k = 2 * ((i >> 1) * 64 + lane) + (i & 1) - (pid & 1) * 320; lds_st<float>(k < 320u ? L_XN + k * 16 + wave * 4 : L_QP + lane * 4, rbf(xn[i])); }
    }
    ctl_arrive(C_XCNT, lane);
    if (hasq) {
      ctl_wait_ge<1>(C_XCNT, 4);
      f32x2 qa[4][4];
#pragma unroll
      for (int b = 0; b < 4; ++b)
#pragma unroll
        for (int e = 0; e < 4; ++e) qa[b][e] = f32x2{0.f, 0.f};
#pragma unroll
      for (int i = 0; i < 10; ++i) {
        const f32x4 x4 = lds_ld<f32x4>(L_XN + (80 * wave + 8 * i + (lane >> 3)) * 16);
#pragma unroll
        for (int e = 0; e < 4; ++e) { const f32x2 w2 = {bflo(wv[i][e]), bfhi(wv[i][e])};
#pragma unroll
          for (int b = 0; b < 4; ++b) qa[b][e] = __builtin_elementwise_fma(w2, f32x2{x4[b], x4[b]}, qa[b][e]); }
      }
      float a[32];
#pragma unroll
      for (int b = 0; b < 4; ++b)
#pragma unroll
        for (int e = 0; e < 4; ++e) { a[b * 8 + 2 * e] = qa[b][e][0]; a[b * 8 + 2 * e + 1] = qa[b][e][1]; }
#pragma unroll
      for (int i = 0; i < 16; ++i) a[i] = sw32_add(a[i], a[i + 16]);
#pragma unroll
      for (int i = 0; i < 8; ++i) a[i] = sw16_add(a[i], a[i + 8]);
      f32x4 r;
#pragma unroll
      for (int i = 0; i < 4; ++i) { const float t1 = a[i] + dpp<ROR8>(a[i]), t2 = a[i + 4] + dpp<ROR8>(a[i + 4]); r[i] = (lane & 8) ? t2 : t1; }
      lds_st<f32x4>(L_QP + (wave * 256 + g4 * 64 + (lane & 7) * 8 + ((lane >> 3) & 1) * 4) * 4, r);
      tag = get_tag();
      if (ctl_arrive(C_QCNT, lane) == 3) {
        f32x2 s[2][4];
#pragma unroll
        for (int j = 0; j < 2; ++j)
#pragma unroll
          for (int w = 0; w < 4; ++w) s[j][w] = lds_ld<f32x2>(L_QP + w * 1024 + j * 512 + lane * 8);
#pragma unroll
        for (int j = 0; j < 2; ++j) { const f32x2 t = (s[j][0] + s[j][1]) + (s[j][2] + s[j][3]); put(rs, WS_QG + pid * 2048 + j * 1024 + voff, tag, asu(t[0]), asu(t[1])); }
        ctl_arrive(C_QREADY, lane);
      }
    } else {
      tag = get_tag();
      if (wave == 0) { ctl_wait_ge<1>(C_XCNT, 4); ctl_arrive(C_QREADY, lane); }
    }
    const u32 koff = (lane & 15) * 384 + (g4 ^ ((lane & 15) >> 1)) * 16;
    const u32 voffs = (4 * g4 + ((lane >> 2) & 3)) * 384 + ((4 * g4 + ((lane >> 2) & 3)) & 6) * 16 + (lane & 3) * 8;
#define LOAD_K(kf_, sl_) do { const u32 ka_ = (sl_) + koff; \
      _Pragma("unroll") for (int c = 0; c < 6; ++c) kf_[c] = lds_ld<u32x4>((ka_ ^ ((c & 1) * 64)) + (c >> 1) * 128); } while (0)
#define LOAD_V(sl_) do { const u32 va_ = (sl_) + voffs; \
      _Pragma("unroll") for (int c = 0; c < 12; ++c) vf[c] = __builtin_bit_cast(u32x2, __builtin_amdgcn_ds_read_tr16_b64_v4i16((LAS s16x4*)((LAS char*)smem + (va_ ^ ((c & 3) * 32)) + (c >> 2) * 128))); } while (0)
#define PIN_V() asm volatile("s_waitcnt lgkmcnt(0)" : "+v"(vf[0]), "+v"(vf[1]), "+v"(vf[2]), "+v"(vf[3]), "+v"(vf[4]), "+v"(vf[5]), "+v"(vf[6]), "+v"(vf[7]), \
      "+v"(vf[8]), "+v"(vf[9]), "+v"(vf[10]), "+v"(vf[11]) :: "memory")
#define PIN_K(kf_) asm volatile("s_waitcnt lgkmcnt(0)" : "+v"(kf_[0]), "+v"(kf_[1]), "+v"(kf_[2]), "+v"(kf_[3]), "+v"(kf_[4]), "+v"(kf_[5]) :: "memory")
#define SCORES(kf_, r_) do { f32x4 s0 = {0.f, 0.f, 0.f, 0.f}, s1 = {0.f, 0.f, 0.f, 0.f}; \
      _Pragma("unroll") for (int c = 0; c < 3; ++c) { s0 = MFMA32(kf_[c], qf[c], s0); s1 = MFMA32(kf_[c + 3], qf[c + 3], s1); } \
      _Pragma("unroll") for (int j = 0; j < 2; ++j) { const u32 r1 = cvtpk(s0[2 * j] + s1[2 * j], s0[2 * j + 1] + s1[2 * j + 1]); \
        r_[j] = cvtpk(bflo(r1) * 0.07216878364870322f, bfhi(r1) * 0.07216878364870322f); } } while (0)
#define MAX4(r_) fmaxf(fmaxf(bflo(r_[0]), bfhi(r_[0])), fmaxf(bflo(r_[1]), bfhi(r_[1])))
#define PV(r_) do { const float p0 = ex2(fmaf(bflo(r_[0]), 1.4426950408889634f, -m2)), p1 = ex2(fmaf(bfhi(r_[0]), 1.4426950408889634f, -m2)), \
        p2 = ex2(fmaf(bflo(r_[1]), 1.4426950408889634f, -m2)), p3 = ex2(fmaf(bfhi(r_[1]), 1.4426950408889634f, -m2)); \
      u32x2 pp = {cvtpk(p0, p1), cvtpk(p2, p3)}; \
      const u32x2 pl = {cvtpk(p0 - bflo(pp[0]), p1 - bfhi(pp[0])), cvtpk(p2 - bflo(pp[1]), p3 - bfhi(pp[1]))}; \
      pp = (lane & 1) ? pl : pp; \
      const s16x4 pa = __builtin_bit_cast(s16x4, pp); \
      _Pragma("unroll") for (int i = 0; i < 12; ++i) acc[i] = __builtin_amdgcn_mfma_f32_16x16x16bf16_1k(pa, __builtin_bit_cast(s16x4, vf[i]), acc[i], 0, 0, 0); \
      l += (p0 + p1) + (p2 + p3); } while (0)
#define WAIT_UNIT(u_) do { if (full <= (u_)) { _Pragma("unroll 1") for (u32 n = 0; n < CAP_LDS; ++n) { full = (int)ctl_ld(C_FULL + 2 * (wave & 1)); if (full > (u_)) break; } \
      asm volatile("" ::: "memory"); } } while (0)
#define NEXT_SLOT(u_, step_) do { const u32 lim = ((u_) < NSW ? NSLOT0 : RS) * 6144u; slot += (step_) * 6144u; if (slot >= lim) slot -= lim; } while (0)
    u32x4 qf[6];
    u32 sr[2][2] = {{0u, 0u}, {0u, 0u}};
    float mloc = -INFINITY;
    u32 mb[3];
    int full = 16;
    u32 slot = (16 + wave) * 6144;
#define K_STEP(kf_, k_) do { u32 r[2]; SCORES(kf_, r); mloc = fmaxf(mloc, MAX4(r)); \
      const bool me_ = (lane & 15) == ((k_) & 15); \
      if ((k_) < 16) { sr[0][0] = me_ ? r[0] : sr[0][0]; sr[0][1] = me_ ? r[1] : sr[0][1]; } else { sr[1][0] = me_ ? r[0] : sr[1][0]; sr[1][1] = me_ ? r[1] : sr[1][1]; } } while (0)
    {
      u32x4 kp[4][6];
      ctl_wait_ge<1>(C_FULL + 2 * (wave & 1), 15);
#pragma unroll
      for (int k = 0; k < 4; ++k) LOAD_K(kp[k], (4 * k + wave) * 6144);
#pragma unroll
      for (int k = 0; k < 4; ++k) PIN_K(kp[k]);
      ctl_st(C_PROG + wave, 4u);
      ctl_wait_ge<1>(C_QREADY, 2);
#pragma unroll
      for (int c = 0; c < 6; ++c) qf[c] = lds_ld<u32x4>(L_QS + c * 64 + g4 * 16);
#pragma unroll
      for (int k = 0; k < 4; ++k) K_STEP(kp[k], k);
    }
#pragma unroll 1
    for (int k = 4; k < NSP / 4; ++k) {
      const int u = 4 * k + wave;
      WAIT_UNIT(u);
      u32x4 kf[6];
      LOAD_K(kf, slot);
      NEXT_SLOT(u, 4);
      PIN_K(kf);
      ctl_st(C_PROG + wave, (u32)(k + 1));
      K_STEP(kf, k);
      if ((k & 7) == 7) {
        const u32 mm = RFL(asu(sw32_max(sw16_max(mloc))));
        if (k == 7) mb[0] = mm; else if (k == 15) mb[1] = mm; else mb[2] = mm;
        mloc = -INFINITY;
#pragma unroll
        for (int c = 0; c < 6; ++c) qf[c] = lds_ld<u32x4>(L_QS + ((k + 1) >> 3) * 384 + c * 64 + g4 * 16);
      }
    }
    float m = asf(mb[0]), m2 = m * 1.4426950408889634f, l = 0.f;
    f32x4 acc[12];
#pragma unroll
    for (int i = 0; i < 12; ++i) acc[i] = f32x4{0.f, 0.f, 0.f, 0.f};
    u32x2 vf[12];
    int b = 0;
#define ACC(i_) (acc[i_][0] + acc[i_][1])
#define WAVE_REC() do { float L = sw16_add(l, l); L = sw32_add(L, L); \
        const u32 wr = L_WREC + (b * 4 + wave) * 776; \
        _Pragma("unroll") for (int j = 0; j < 3; ++j) lds_st<float>(wr + (j * 64 + lane) * 4, pick4(f32x4{ACC(4 * j), ACC(4 * j + 1), ACC(4 * j + 2), ACC(4 * j + 3)}, g4)); \
        if (lane == 0) lds_st<f32x2>(wr + 768, f32x2{m2, L}); } while (0)
#pragma unroll 1
    for (int k = 0; k < NSP / 4; ++k) {
      const int u = NSP + 4 * k + wave;
      const int from = ((lane & 48) | (k & 15)) * 4;
      const u32 r[2] = {(u32)__builtin_amdgcn_ds_bpermute(from, (int)(k < 16 ? sr[0][0] : sr[1][0])), (u32)__builtin_amdgcn_ds_bpermute(from, (int)(k < 16 ? sr[0][1] : sr[1][1]))};
      WAIT_UNIT(u);
      LOAD_V(slot);
      NEXT_SLOT(u, 4);
      PIN_V();
      ctl_st(C_PROG + wave, (u32)(NSP / 4 + k + 1));
      PV(r);
      if ((k & 7) == 7) {
        WAVE_REC();
        ctl_arrive(C_FCNT + b, lane);
        ++b;
        m = b == 1 ? asf(mb[1]) : b == 2 ? asf(mb[2]) : -INFINITY; m2 = m * 1.4426950408889634f; l = 0.f;
#pragma unroll
        for (int i2 = 0; i2 < 12; ++i2) acc[i2] = f32x4{0.f, 0.f, 0.f, 0.f};
      }
    }
    slot = ((UD - NSW + 2 * wave) % RS) * 6144;
    int fullA = 0, fullB = 0, endA = 0x7fffffff;
#pragma unroll 1
    for (int d = 0;; ++d) {
      const int u = UD + 8 * d + 2 * wave;
      bool last = endA <= u;
      if (!last && (fullA <= u || fullB <= u + 1)) {
#pragma unroll 1
        for (u32 n = 0; n < CAP_LDS; ++n) {
          const u32x4 fe = *(volatile LAS u32x4*)((LAS char*)smem + L_CTRL + C_FULL * 4);
          fullA = RFL(fe[0]); fullB = RFL(fe[2]); endA = RFL(fe[1]);
          if (endA <= u || (fullA > u && fullB > u + 1)) break;
        }
        last = fullA <= u || fullB <= u + 1;
        asm volatile("" ::: "memory");
      }
      if (last) break;
      u32x4 kf[6];
      LOAD_K(kf, slot);
      LOAD_V(slot + 6144);
      NEXT_SLOT(u, 8);
      u32 r[2];
      SCORES(kf, r);
      PIN_V();
      ctl_st(C_PROG + wave, (u32)(NSP / 2 + d + 1));
      const float mx = MAX4(r);
      if (__any(mx > m)) {
        const float mn = sw32_max(sw16_max(fmaxf(mx, m))), mn2 = mn * 1.4426950408889634f, al = ex2(m2 - mn2);
        m = mn; m2 = mn2; l *= al;
#pragma unroll
        for (int i = 0; i < 12; ++i) { acc[i][0] *= al; acc[i][1] *= al; }
      }
      PV(r);
    }
    WAVE_REC();
    if (ctl_arrive(C_FCNT + 3, lane) == 3) publish_rec(rs, blockIdx.x, lane, tag);
  } else {
    const int hw = wave - 6;
    if (hw == 0) ctl_st(lane, (lane == C_END || lane == C_END + 2) ? 0x7fffffffu : lane >= C_FIFO ? 0xffffffffu : 0u);
    BAR();
    if (hw == 0) {
      const u32 ep = __hip_atomic_load((const GAS u32*)(ws + WS_EPOCH), RLX_AGENT);
      tag = (RFL(ep) + 0x9E3779B9u) | 1u;
      ctl_st(C_TAG, tag);
      if (xx == 0) {
        wait_pos(40);
        if (lane == 0) __hip_atomic_store((GAS u32*)(ws + WS_TICK + g * 256), 0u, RLX_AGENT);
        VM0();
        put(rs, lane == 0 ? WS_TICK + g * 256 + 128 : OOB, tag, 0u, 0u);
      }
    } else {
      tag = get_tag();
      ctl_wait_ge<2>(C_QREADY, 1);
      __builtin_amdgcn_s_sleep(20);
      u32 o[12], a[12], b[12];
#pragma unroll
      for (int j = 0; j < 12; ++j) o[j] = WS_QG + (6 * g + 2 * (j % 3) + (lane >> 5)) * 2048 + (j / 3) * 512 + (lane & 31) * 16;
      sweep<12, 8>(rs, o, tag, a, b);
#pragma unroll
      for (int j = 0; j < 12; ++j) { const float q = sw32_add(asf(a[j]), asf(b[j]));
        lds_st<u16>(L_QS + j * 128 + (lane & 31) * 4 + (lane >> 5) * 2, (u16)cvtpk(q, q)); }
      ctl_arrive(C_QREADY, lane);
    }
#define ROLE_W(first_, step_) do { \
      const int nrow = role_c ? 20 : 24, ld = role_c ? F * 2 : D * 2; \
      const i32x4 r0 = mkrsrc(role_c ? wgate + 192 * xx + 16 * gr : wdown + 16 * ri, D * F * 2); \
      const i32x4 r1 = mkrsrc(role_c ? wup + 192 * xx + 16 * gr : wdown + 16 * ri + 768 * D, role_c ? D * F * 2 : D * F); \
      const u32 vo = (lane >> 1) * ld + (lane & 1) * 16; int cnt = 0; \
      _Pragma("unroll 1") for (int i0 = (first_); i0 < nrow; i0 += (step_)) { const int i1 = i0 + (gr & 3) * (nrow >> 2), i = i1 >= nrow ? i1 - nrow : i1; \
        DMA1(vo, r0, L_WR + i * 1024, i * 32 * ld); DMA1(vo, r1, L_WR + (nrow + i) * 1024, i * 32 * ld); if (++cnt == 2) { cnt = 0; VM0(); } } \
      VM0(); } while (0)
    if (role_c || role_d) { ctl_wait_ge<8>(C_FULL, 66); ctl_wait_ge<8>(C_FULL + 2, 66); ROLE_W(hw, 2); }
    if (hw == 1) {
      ctl_wait_ge<8>(C_FCNT + 2, 4);
#pragma unroll 1
      for (int b = 0; b < 3; ++b) publish_early(rs, b, blockIdx.x, lane);
      VM0();
      put(rs, lane == 0 ? WS_REC0 + blockIdx.x * ER_SZ + 1280 : OOB, tag, 0u, 0u);
      wait_pos(UD + 24);
      { u32 o[1] = {lane < 8 ? WS_REC0 + (hp * 8 + lane) * ER_SZ + 1280 : OOB}, a[1], c[1]; sweep<1, 16>(rs, o, tag, a, c); }
      gather_early(rs, hp, lane);
    }
    if (hw == 0) {
      ctl_wait_ge<8>(C_FULL, UD - 8);
      { u32 o[1] = {WS_TICK + g * 256 + 128}, a[1], c[1]; sweep<1, 32>(rs, o, tag, a, c); }
      const rsrc_t rt = __builtin_amdgcn_make_buffer_rsrc(ws + WS_TICK + g * 256, 0, 4, 0x00020000);
      int n = NSD, cap = 6;
#pragma unroll 1
      for (;;) {
        ctl_wait_ge<1>(C_FULL, UD + 2 * n - LOOK - 2 * LAG + 1);
        int c = (((int)ctl_ld(C_FULL) + 2 * LAG + LOOK - UD) >> 1) - n;
        c = c < 1 ? 1 : c > cap ? cap : c;
        const u32 base = RFL(__builtin_amdgcn_raw_ptr_buffer_atomic_add_i32(c, rt, voff, 0, 0));
        if (lane <= c) { const u32 t = 768 + 8 * NSD + base + lane;
          if (lane < c || t >= 1024u) ctl_st(C_FIFO + ((n + lane) & 31), ((u32)(n + lane) << 16) | (t < 1024u ? t : 0xffffu)); }
        if (base + c >= (u32)NDYN) break;
        n += c;
        cap = (NDYN - (int)base) / 16; cap = cap < 2 ? 2 : cap > 6 ? 6 : cap;
      }
      const i32x4 rw = mkrsrc(wo + (size_t)hp * DH * D + cs * 80, DH * D * 2);
      u32 nn = lane;
#pragma unroll 1
      for (int i = 0; i < 30; ++i, nn += 64) {
        const u32 row = (nn * 52429u) >> 19, vo = row * (D * 2 - 160) + nn * 16;
        DMA1(vo, rw, L_WO + i * 1024, 0);
      }
    }
  }
  if (wave >= 5) {
    ctl_wait_ge<1>(C_FCNT + 3, 1);
    const u32 o = combine<2>(rs, WS_REC3 + hp * 8 * REC_SZ, wave - 5, lane, tag);
    lds_st<u32>(L_OS + 3 * 448 + (wave - 5) * 128 + (lane & 31) * 4, o);
    VM0();
  }
  BAR();
  const u32 trow = g4 * 8 + ((lane >> 2) & 3), tcol = (lane & 3) * 8;
  if (wave < 5) {
    f32x4 c = {0.f, 0.f, 0.f, 0.f}, c2 = {0.f, 0.f, 0.f, 0.f};
    const u32 ab = L_OS + (lane & 3) * 448 + g4 * 16, bb = L_WO + trow * 160 + tcol + wave * 32;
#pragma unroll
    for (int s = 0; s < 3; ++s) { c = MFMA32(lds_ld<u32x4>(ab + s * 64), trfrag(bb + s * 5120, 640), c);
      c2 = MFMA32(lds_ld<u32x4>(ab + 192 + s * 64), trfrag(bb + 15360 + s * 5120, 640), c2); }
    const float v = pick4(c + c2, g4), nb = dpp<XOR1>(v);
    put(rs, (lane & 1) ? OOB : WS_PG + ((cs * 5 + wave) * 32 + hp) * 512 + lane * 8, tag, asu(v), asu(nb));
  }
  if (role_c) {
    u32x4 wf[5];
    { const u32 bb = L_WR + (wave & 1) * 20480 + (wave >> 1) * 5120 + trow * 32 + tcol;
#pragma unroll
      for (int s = 0; s < 5; ++s) wf[s] = trfrag(bb + s * 1024, 128); }
    if (wave < 4) {
      u32 o[3], a[3], b[3];
#pragma unroll
      for (int i = 0; i < 3; ++i) { const u32 n = i * 64 + lane; o[i] = n < 160 ? WS_HG + (xx % RH) * HG_SZ + (n >> 2) * 256 + wave * 64 + (n & 3) * 16 : OOB; }
      sweep<3, 1>(rs, o, tag, a, b);
      float hv[12];
#pragma unroll
      for (int i = 0; i < 3; ++i) { hv[4 * i] = bflo(a[i]); hv[4 * i + 1] = bfhi(a[i]); hv[4 * i + 2] = bflo(b[i]); hv[4 * i + 3] = bfhi(b[i]); }
      lnorm<12>(hv, lane < 32);
#pragma unroll
      for (int i = 0; i < 3; ++i) if (i < 2 || lane < 32)
        lds_st<u32x2>(L_HN + wave * 1344 + (i * 64 + lane) * 8, u32x2{cvtpk(hv[4 * i], hv[4 * i + 1]), cvtpk(hv[4 * i + 2], hv[4 * i + 3])});
    }
    BAR();
    {
      f32x4 c = {0.f, 0.f, 0.f, 0.f}, c2 = {0.f, 0.f, 0.f, 0.f};
      const u32 ab = L_HN + (lane & 3) * 1344 + g4 * 16 + (wave >> 1) * 320;
#pragma unroll
      for (int s = 0; s < 5; ++s) { if (s & 1) c2 = MFMA32(lds_ld<u32x4>(ab + s * 64), wf[s], c2); else c = MFMA32(lds_ld<u32x4>(ab + s * 64), wf[s], c); }
      lds_st<float>(L_RED + (wave * 64 + lane) * 4, pick4(c + c2, g4));
    }
    BAR();
    if (wave == 0) {
      float r8[8];
#pragma unroll
      for (int w = 0; w < 8; ++w) r8[w] = lds_ld<float>(L_RED + (w * 64 + lane) * 4);
      const float gt = rbf((r8[0] + r8[2]) + (r8[4] + r8[6])), up = rbf((r8[1] + r8[3]) + (r8[5] + r8[7]));
      const float sg = rbf(gt * __builtin_amdgcn_rcpf(1.f + __expf(-gt)));
      put_row4(rs, WS_AG + (12 * xx + gr) * 256, AG_SZ, RA, lane, tag, sg * up);
    }
  } else if (role_d) {
    const float xv = bflo(((const GAS u16*)xin)[g4 * D + 16 * ri + (lane & 15)]);
    u32x4 wf[6];
    { const u32 bb = L_WR + wave * 6144 + trow * 32 + tcol;
#pragma unroll
      for (int s = 0; s < 6; ++s) wf[s] = trfrag(bb + s * 1024, 128); }
    {
      u32 o[2], a[2], b[2];
#pragma unroll
      for (int i = 0; i < 2; ++i) o[i] = WS_PG + (ri * 32 + 4 * wave) * 512 + i * 1024 + voff;
      sweep<2, 1>(rs, o, tag, a, b);
      lds_st<float>(L_RED + (wave * 64 + 2 * (lane & 31) + (lane >> 5)) * 4, sw32_add(asf(a[0]) + asf(a[1]), asf(b[0]) + asf(b[1])));
    }
    BAR();
    float hval = 0.f;
    if (wave == 0) {
      float r8[8];
#pragma unroll
      for (int w = 0; w < 8; ++w) r8[w] = lds_ld<float>(L_RED + (w * 64 + lane) * 4);
      hval = rbf(xv + rbf(((r8[0] + r8[1]) + (r8[2] + r8[3])) + ((r8[4] + r8[5]) + (r8[6] + r8[7]))));
      put_row4(rs, WS_HG + ri * 256, HG_SZ, RH, lane, tag, hval);
    }
    {
      u32 o[3], a[3], b[3];
#pragma unroll
      for (int i = 0; i < 3; ++i) o[i] = WS_AG + (xx % RA) * AG_SZ + wave * 3072 + i * 1024 + voff;
      sweep<3, 1>(rs, o, tag, a, b);
#pragma unroll
      for (int i = 0; i < 3; ++i) lds_st<u32x2>(L_AS + wave * 1792 + ((lane >> 2) & 3) * 448 + (4 * i + g4) * 32 + (lane & 3) * 8, u32x2{a[i], b[i]});
      f32x4 c = {0.f, 0.f, 0.f, 0.f}, c2 = {0.f, 0.f, 0.f, 0.f};
      const u32 ab = L_AS + wave * 1792 + (lane & 3) * 448 + g4 * 16;
#pragma unroll
      for (int s = 0; s < 3; ++s) { c = MFMA32(lds_ld<u32x4>(ab + s * 64), wf[s], c); c2 = MFMA32(lds_ld<u32x4>(ab + 192 + s * 64), wf[s + 3], c2); }
      lds_st<float>(L_RED + 2048 + (wave * 64 + lane) * 4, pick4(c + c2, g4));
    }
    BAR();
    if (wave == 0) {
      float r8[8];
#pragma unroll
      for (int w = 0; w < 8; ++w) r8[w] = lds_ld<float>(L_RED + 2048 + (w * 64 + lane) * 4);
      const float ov = hval + rbf(((r8[0] + r8[1]) + (r8[2] + r8[3])) + ((r8[4] + r8[5]) + (r8[6] + r8[7]))), nb = dpp<XOR1>(ov);
      if (!(lane & 1)) ((GAS u32*)out)[(g4 * D + 16 * ri + (lane & 15)) >> 1] = cvtpk(ov, nb);
      if (ri == 0 && lane == 0) __hip_atomic_store((GAS u32*)(ws + WS_EPOCH), tag, RLX_AGENT);
    }
  }
}

extern "C" void kernel_launch(void* const* d_in, const int* in_sizes, int n_in, void* d_out, int out_size, void* d_ws, size_t ws_size,
                              hipStream_t stream) {
  fused_decode<<<256, 512, 0, stream>>>((const u16*)d_in[0], (const u16*)d_in[1], (const u16*)d_in[2], (const u16*)d_in[3], (const u16*)d_in[4],
                                        (const u16*)d_in[6], (const u16*)d_in[7], (const u16*)d_in[8], (u16*)d_out, (char*)d_ws);
}
```
